```python
import torch

def _softmax(x, axis=-1):
    return torch.softmax(x, dim=axis)
def _gelu(x):
    return torch.nn.functional.gelu(x, approximate="tanh")
def _silu(x):
    return torch.nn.functional.silu(x)
def _sigmoid(x):
    return torch.sigmoid(x)
def _layernorm(x, axis=-1, eps=1e-5):
    m = x.mean(dim=axis, keepdim=True)
    v = x.var(dim=axis, keepdim=True, unbiased=False)
    return (x - m) / torch.sqrt(v + eps)
def _scatter_add(tgt, idx, src, axis=0):
    return tgt.clone().index_add_(axis, idx, src)
def _segment_sum(src, seg, n):
    out = torch.zeros((n,) + src.shape[1:], dtype=src.dtype)
    return out.index_add_(0, seg, src)
def _pad(x, pw):
    flat = tuple(v for pair in reversed(pw) for v in pair)
    return torch.nn.functional.pad(x, flat)

kernel_name = 'standalone__softmax_full_bidir__sampling'

def setup_inputs():

    _w = (lambda a: (lambda t: t.to(torch.bfloat16) if t.is_floating_point() else t)(torch.as_tensor(a)))
    B, H, Sq, Skv, Dh = 16, 32, 4, 4096, 128
    return {
        "q": _w((lambda *s: torch.randn(*s, dtype=torch.bfloat16) * 0.8)(B, H, Sq, Dh)),
        "k": _w((lambda *s: torch.randn(*s, dtype=torch.bfloat16) * 0.8)(B, H, Skv, Dh)),
        "v": _w((lambda *s: torch.randn(*s, dtype=torch.bfloat16) * 0.05)(B, H, Skv, Dh)),
    }

def reference(q, k, v):
    scale = q.shape[-1] ** -0.5
    scores = torch.einsum('bhqd,bhkd->bhqk', q, k) * scale
    attn = _softmax(scores, axis=-1)
    return torch.einsum('bhqk,bhkd->bhqd', attn, v)
```

```cpp
#include <hip/hip_runtime.h>
#include <hip/hip_bf16.h>
#include <cstdint>

constexpr int D = 128, SKV = 4096, SQ = 4, NBH = 512, WAVES = 4, NTHR = WAVES * 64, PAGE = 16;
constexpr float SCALE = 0.088388347648318440f;
using bf16x2 = __attribute__((ext_vector_type(2))) __bf16;
using u32x4  = __attribute__((ext_vector_type(4))) unsigned;
using f32x4  = __attribute__((ext_vector_type(4))) float;
__device__ __forceinline__ float dot2(unsigned k, unsigned q, float acc) {
  return __builtin_amdgcn_fdot2_f32_bf16(__builtin_bit_cast(bf16x2, k), __builtin_bit_cast(bf16x2, q), acc, false);
}
template <int CTRL> __device__ __forceinline__ float dpp(float x) {
  return __builtin_bit_cast(float, __builtin_amdgcn_mov_dpp(__builtin_bit_cast(int, x), CTRL, 0xf, 0xf, true));
}
constexpr int XOR1 = 0xB1, XOR2 = 0x4E, XOR7 = 0x141, XOR8 = 0x128, ROR4 = 0x124;
__device__ __forceinline__ float xrow16_max(float x) {
  auto s = __builtin_amdgcn_permlane16_swap(__float_as_uint(x), __float_as_uint(x), false, false);
  x = fmaxf(__uint_as_float(s[0]), __uint_as_float(s[1]));
  auto t = __builtin_amdgcn_permlane32_swap(__float_as_uint(x), __float_as_uint(x), false, false);
  return fmaxf(__uint_as_float(t[0]), __uint_as_float(t[1]));
}
__device__ __forceinline__ float xrow16_sum(float x) {
  auto s = __builtin_amdgcn_permlane16_swap(__float_as_uint(x), __float_as_uint(x), false, false);
  x = __uint_as_float(s[0]) + __uint_as_float(s[1]);
  auto t = __builtin_amdgcn_permlane32_swap(__float_as_uint(x), __float_as_uint(x), false, false);
  return __uint_as_float(t[0]) + __uint_as_float(t[1]);
}
constexpr int LDS_P = 0, LDS_MRG = WAVES * 64, MRG_REC = 4 + D, LDS_FLOATS = LDS_MRG + WAVES * 4 * MRG_REC;

__global__ void __launch_bounds__(NTHR) attn_k(const unsigned short* __restrict__ q, const unsigned short* __restrict__ k, const unsigned short* __restrict__ v, __hip_bfloat16* __restrict__ out) {
  __shared__ __attribute__((aligned(16))) float lds[LDS_FLOATS];
  const int bh = blockIdx.x;
  const int tid = threadIdx.x, lane = tid & 63, wave = __builtin_amdgcn_readfirstlane(tid >> 6), r = lane >> 4, c = lane & 15;
  constexpr float C2 = SCALE * 1.4426950408889634f;
  constexpr int NP = SKV / PAGE / WAVES;
  u32x4 qpk[4];
#pragma unroll
  for (int g = 0; g < 4; ++g) qpk[g] = *reinterpret_cast<const u32x4*>(q + ((size_t)bh * SQ + g) * D + 8 * c);
  const size_t base = (size_t)bh * SKV * D + (size_t)r * D + 8 * c;
#define ISSUE(i_) do { const unsigned short* kb_ = k + base + (size_t)(wave + 4 * (i_)) * PAGE * D; const unsigned short* vb_ = v + base + (size_t)(wave + 4 * (i_)) * PAGE * D; \
    _Pragma("unroll") for (int t_ = 0; t_ < 4; ++t_) { Kn[t_] = __builtin_nontemporal_load(reinterpret_cast<const u32x4*>(kb_ + t_ * 4 * D)); Vn[t_] = __builtin_nontemporal_load(reinterpret_cast<const u32x4*>(vb_ + t_ * 4 * D)); } } while (0)
  u32x4 Kn[4], Vn[4];
  float m = -INFINITY, l = 0.f, acc[32];
#pragma unroll
  for (int kk = 0; kk < 32; ++kk) acc[kk] = 0.f;
  ISSUE(0);
  for (int i = 0; i < NP; ++i) {
    u32x4 Kc[4], Vc[4];
#pragma unroll
    for (int t = 0; t < 4; ++t) { Kc[t] = Kn[t]; Vc[t] = Vn[t]; }
    if (i + 1 < NP) ISSUE(i + 1);
    float x[16];
#pragma unroll
    for (int t = 0; t < 4; ++t)
#pragma unroll
      for (int g = 0; g < 4; ++g) { float d = 0.f;
#pragma unroll
          for (int kk = 0; kk < 4; ++kk) d = dot2(Kc[t][kk], qpk[g][kk], d);
        x[t * 4 + g] = d; }
#define TR_STEP(HALF, CTRL, BIT) _Pragma("unroll") for (int h = 0; h < (HALF); ++h) { \
      const float lo_ = x[h], hi_ = x[h + (HALF)]; const float t1_ = lo_ + dpp<CTRL>(lo_), t2_ = hi_ + dpp<CTRL>(hi_); \
      x[h] = (c & (BIT)) ? t2_ : t1_; }
    TR_STEP(8, XOR8, 8) TR_STEP(4, XOR7, 4) TR_STEP(2, XOR2, 2) TR_STEP(1, XOR1, 1)
#undef TR_STEP
    const float sc = x[0] * C2;
    float pm = sc; pm = fmaxf(pm, dpp<ROR4>(pm)); pm = fmaxf(pm, dpp<XOR8>(pm)); pm = xrow16_max(pm);
    const float mn = fmaxf(m, pm);
    const float alpha = __builtin_amdgcn_exp2f(m - mn), p = __builtin_amdgcn_exp2f(sc - mn);
    l = fmaf(l, alpha, p); m = mn;
#pragma unroll
    for (int q4 = 0; q4 < 4; ++q4) {
        const float al = q4 == 0 ? dpp<0x00>(alpha) : q4 == 1 ? dpp<0x55>(alpha) : q4 == 2 ? dpp<0xAA>(alpha) : dpp<0xFF>(alpha);
#pragma unroll
        for (int kk = 0; kk < 8; ++kk) acc[q4 * 8 + kk] *= al; }
    float* prow = lds + LDS_P + (wave * 4 + r) * 16;
    prow[c] = p;
    asm volatile("s_waitcnt lgkmcnt(0)" ::: "memory");
    f32x4 pr[4];
#pragma unroll
    for (int u = 0; u < 4; ++u) pr[u] = *reinterpret_cast<const f32x4*>(prow + 4 * u);
#pragma unroll
    for (int t = 0; t < 4; ++t) {
      float vf[8];
#pragma unroll
      for (int kk = 0; kk < 4; ++kk) { vf[2 * kk] = __builtin_bit_cast(float, Vc[t][kk] << 16); vf[2 * kk + 1] = __builtin_bit_cast(float, Vc[t][kk] & 0xffff0000u); }
#pragma unroll
      for (int g = 0; g < 4; ++g) { const float pg_ = pr[t][g];
#pragma unroll
        for (int kk = 0; kk < 8; ++kk) acc[g * 8 + kk] = fmaf(pg_, vf[kk], acc[g * 8 + kk]); } }
  }
#undef ISSUE
#pragma unroll
  for (int kk = 0; kk < 32; ++kk) acc[kk] = xrow16_sum(acc[kk]);
  { float t = l; t += dpp<ROR4>(t); t += dpp<XOR8>(t); l = xrow16_sum(t); }
  float* mrg = lds + LDS_MRG;
  if (r == 0) {
#pragma unroll
    for (int g = 0; g < 4; ++g) {
      float* rec = mrg + (wave * 4 + g) * MRG_REC;
#pragma unroll
      for (int kk = 0; kk < 8; kk += 4) *reinterpret_cast<f32x4*>(rec + 4 + 8 * c + kk) = f32x4{acc[g * 8 + kk], acc[g * 8 + kk + 1], acc[g * 8 + kk + 2], acc[g * 8 + kk + 3]};
      if (c == g) { rec[0] = m; rec[1] = l; } } }
  __syncthreads();
  {
    const int g = wave, d0 = 2 * lane;
    float M = -INFINITY;
#pragma unroll
    for (int w = 0; w < WAVES; ++w) M = fmaxf(M, mrg[(w * 4 + g) * MRG_REC]);
    float L = 0.f, o0 = 0.f, o1 = 0.f;
#pragma unroll
    for (int w = 0; w < WAVES; ++w) { const float* rec = mrg + (w * 4 + g) * MRG_REC; const float lw = rec[1];
      const float wt = __builtin_amdgcn_exp2f(rec[0] - M);
      L = fmaf(wt, lw, L); o0 = fmaf(wt, rec[4 + d0], o0); o1 = fmaf(wt, rec[4 + d0 + 1], o1); }
    const float inv = 1.f / L;
    __hip_bfloat162 o; o.x = __float2bfloat16(o0 * inv); o.y = __float2bfloat16(o1 * inv);
    *reinterpret_cast<__hip_bfloat162*>(out + ((size_t)bh * SQ + g) * D + d0) = o;
  }
}

extern "C" void kernel_launch(void* const* d_in, const int* in_sizes, int n_in, void* d_out, int out_size, void* d_ws, size_t ws_size, hipStream_t stream) {
  attn_k<<<NBH, NTHR, 0, stream>>>((const unsigned short*)d_in[0], (const unsigned short*)d_in[1], (const unsigned short*)d_in[2], (__hip_bfloat16*)d_out);
}
```

```cpp
#include <hip/hip_runtime.h>
#include <cstdint>

namespace {
constexpr int D = 128, SKV = 4096, SQ = 4, NBH = 512, HPW = 2;
constexpr int SCORE_WORDS = HPW * SKV * SQ + 256;
constexpr int NPUB = 8, MS0 = 4, STEP = 12, DLY = 4, PEND = 126, TAU0 = 110, TH = 15, NEAR = 2000;
constexpr float FILT = 0.7f, CLMP = 3.f;
using u32x4 = __attribute__((ext_vector_type(4))) unsigned;
#define LAS __attribute__((address_space(3)))
template <int CTRL> __device__ __forceinline__ int dpp(int x) { return __builtin_amdgcn_mov_dpp(x, CTRL, 0xf, 0xf, true); }
__device__ __forceinline__ int sum16(int x) { x += dpp<0xB1>(x); x += dpp<0x4E>(x); x += dpp<0x141>(x); x += dpp<0x128>(x); return __builtin_amdgcn_readfirstlane(x); }

__global__ void __launch_bounds__(128, 1) attn_k(const char* k, unsigned kn, unsigned kc, const char* q, unsigned qn, unsigned qc, const char* v, char* out, char* ws) {
  __shared__ __attribute__((aligned(16))) float lds[SCORE_WORDS + 4];
  asm volatile(R"ASM(
    ; ---- vector registers
    .set .LT0, 1      ; temporaries
    .set .LT1, 2
    .set .LOO, 3      ; lane * 16
    .set .LKO, 4      ; 4 per-lane offsets of a tile's loads
    .set .LRG, 8      ; ring: 32 loads x 4
    .set .LQR, 136    ; Q fragments [head][line] x 4
    .set .LBQ, 152    ; B operands [line][tile of the pair] x 4
    .set .LDA, 168    ; scores of keys 0..7  [pair] x 4
    .set .LDB, 176    ; scores of keys 8..15 [pair] x 4
    .set .LRR, 184    ; scores of 4 tiles
    .set .LSC, 188    ; scores read back [2] x 4
    .set .LTT, 196    ; packed p
    .set .LPA, 200    ; A operands of P V, 4 x 2
    .set .LA0, 208    ; O[q][8c + 0..3]
    .set .LA1, 212    ; O[q][8c + 4..7]
    .set .LO0, 216    ; packed output of head 0
    .set .LPG, 220    ; {round, time}
    .set .LMX, 222
    .set .LM0, 223    ; max * c of the head in work
    .set .LM1, 224    ; max * c of head 1
    .set .LLL, 226    ; l
    .set .LRL, 227    ; 1 / l
    .set .LLW, 228    ; LDS write address
    .set .LLR, 229    ; LDS read address
    .set .LMKX, 230
    .set .LMKY, 231
    .set .LMD, 232    ; pacing mode
    .set .LCA, 233    ; LDS address of {round, time, mode}
    ; ---- scalar registers: s[4:7] K (later V) descriptor, s[8:11] Q descriptor, s[12:13] v, s[14:15] out, s16 tile offset, s17 rounds left of this head,
    ;      s18 phase / rounds left, s19 mode, s20 c = log2(e)/sqrt(D), s21 temporary, s[22:23] clock, s[24:25] lanes of tile 0 of a pair

    .macro LOAD m, off            ; ring slot m <- 1 KiB at tile offset s16
      buffer_load_dwordx4 v[.LRG+4*(\m):.LRG+4*(\m)+3], v[.LKO+((\m)&3)], s[4:7], s16 offen nt
      .if ((\m)&3) == 3
        s_addk_i32 s16, 0x1000
      .endif
    .endm
    .macro LOAD0 t                ; first K tiles, before the four offsets exist
      buffer_load_dwordx4 v[.LRG+16*(\t):.LRG+16*(\t)+3], v[.LKO], s[4:7], s16 offen nt
      buffer_load_dwordx4 v[.LRG+16*(\t)+4:.LRG+16*(\t)+7], v[.LKO], s[4:7], s16 offen offset:128 nt
      buffer_load_dwordx4 v[.LRG+16*(\t)+8:.LRG+16*(\t)+11], v[.LKO], s[4:7], s16 offen offset:2048 nt
      buffer_load_dwordx4 v[.LRG+16*(\t)+12:.LRG+16*(\t)+15], v[.LKO], s[4:7], s16 offen offset:2176 nt
      s_addk_i32 s16, 0x1000
    .endm
    .macro GATE                   ; in front of a tile: its 4 loads have landed; pacing: at most 28 / 20 / 17 loads in flight (mode 0 / 1 / 2) before 4 more go out
      s_cbranch_vccnz 1f
      s_waitcnt vmcnt(20)
      s_cmp_eq_u32 s19, 1
      s_cbranch_scc1 1f
      s_waitcnt vmcnt(17)
    1:
      s_waitcnt vmcnt(28)
    .endm
    .macro ROUND                  ; start of a round: report {round, time}, take the mode
      s_waitcnt lgkmcnt(0)
      v_readfirstlane_b32 s19, v[.LMD]
      v_add_u32 v[.LPG], 1, v[.LPG]
      v_mov_b32 v[.LPG+1], s22
      ds_write_b64 v[.LCA], v[.LPG:.LPG+1]
    .endm
    .macro MID
      s_memrealtime s[22:23]
      ds_read_b32 v[.LMD], v[.LCA] offset:8
    .endm
    .macro XROWS op, r            ; r = op over the four 16-lane rows
      v_mov_b32 v[.LT1], v[\r]
      s_nop 1
      v_permlane16_swap_b32 v[\r], v[.LT1]
      s_nop 1
      \op v[\r], v[\r], v[.LT1]
      v_mov_b32 v[.LT1], v[\r]
      s_nop 1
      v_permlane32_swap_b32 v[\r], v[.LT1]
      s_nop 1
      \op v[\r], v[\r], v[.LT1]
    .endm
    .macro KTILE m, pair, tp      ; 16 keys: ring slots m..m+3
      GATE
      .if \tp == 0
        v_mfma_f32_16x16x32_bf16 v[.LDA+4*\pair:.LDA+4*\pair+3], v[.LRG+4*(\m):.LRG+4*(\m)+3], v[.LBQ:.LBQ+3], 0
      .else
        v_mfma_f32_16x16x32_bf16 v[.LDA+4*\pair:.LDA+4*\pair+3], v[.LRG+4*(\m):.LRG+4*(\m)+3], v[.LBQ+4:.LBQ+7], v[.LDA+4*\pair:.LDA+4*\pair+3]
      .endif
      LOAD \m
      v_mfma_f32_16x16x32_bf16 v[.LDA+4*\pair:.LDA+4*\pair+3], v[.LRG+4*(\m)+4:.LRG+4*(\m)+7], v[.LBQ+8+4*\tp:.LBQ+11+4*\tp], v[.LDA+4*\pair:.LDA+4*\pair+3]
      LOAD \m+1
      .if \tp == 0
        v_mfma_f32_16x16x32_bf16 v[.LDB+4*\pair:.LDB+4*\pair+3], v[.LRG+4*(\m)+8:.LRG+4*(\m)+11], v[.LBQ:.LBQ+3], 0
      .else
        v_mfma_f32_16x16x32_bf16 v[.LDB+4*\pair:.LDB+4*\pair+3], v[.LRG+4*(\m)+8:.LRG+4*(\m)+11], v[.LBQ+4:.LBQ+7], v[.LDB+4*\pair:.LDB+4*\pair+3]
      .endif
      LOAD \m+2
      v_mfma_f32_16x16x32_bf16 v[.LDB+4*\pair:.LDB+4*\pair+3], v[.LRG+4*(\m)+12:.LRG+4*(\m)+15], v[.LBQ+8+4*\tp:.LBQ+11+4*\tp], v[.LDB+4*\pair:.LDB+4*\pair+3]
      LOAD \m+3
    .endm
    .macro KPAIR m, pair          ; two tiles -> columns 8*pair..8*pair+7 of RR
      KTILE \m, \pair, 0
      KTILE \m+4, \pair, 1
      s_nop 5
      .irp x, 0, 1, 2, 3
        v_permlane32_swap_b32 v[.LDA+4*\pair+\x], v[.LDB+4*\pair+\x]
      .endr
      s_nop 1
      .irp x, 0, 1, 2, 3
        .if \pair == 0
          v_add_f32_dpp v[.LRR+\x], v[.LDB+\x], v[.LDA+\x] row_ror:8 row_mask:0xf bank_mask:0x3
        .else
          v_add_f32_dpp v[.LRR+\x], v[.LDA+4+\x], v[.LDB+4+\x] row_ror:8 row_mask:0xf bank_mask:0xc
        .endif
      .endr
    .endm
    .macro KGROUP g               ; 64 keys: lane (row r, column 8*pair + 4*tile + q), register x = score of key 4r+x
      KPAIR 16*\g, 0
      KPAIR 16*\g+8, 1
      ds_write_b128 v[.LLW], v[.LRR:.LRR+3] offset:1024*\g
      v_max3_f32 v[.LMX], v[.LMX], v[.LRR], v[.LRR+1]
      v_max3_f32 v[.LMX], v[.LMX], v[.LRR+2], v[.LRR+3]
    .endm
    .macro PGROUP g               ; scores of 64 keys, lane (row r, column 4q + tile) -> packed p, l
      .irp x, 0, 1, 2, 3
        v_fma_f32 v[.LSC+4*\g+\x], v[.LSC+4*\g+\x], s20, -v[.LM0]
      .endr
      .irp x, 0, 1, 2, 3
        v_exp_f32 v[.LSC+4*\g+\x], v[.LSC+4*\g+\x]
      .endr
      .irp x, 0, 1, 2, 3
        v_cvt_pk_bf16_f32 v[.LTT+\x], v[.LSC+4*\g+\x], v[.LSC+4*\g+\x]
      .endr
      v_add_f32 v[.LSC+4*\g], v[.LSC+4*\g], v[.LSC+4*\g+1]
      v_add_f32 v[.LSC+4*\g+2], v[.LSC+4*\g+2], v[.LSC+4*\g+3]
      v_add_f32 v[.LLL], v[.LLL], v[.LSC+4*\g]
      v_add_f32 v[.LLL], v[.LLL], v[.LSC+4*\g+2]
    .endm
    .macro VTILE g, t, last       ; 16 keys: ring slots 16g+4t .. +3; the last round only drains the ring
      .irp x, 0, 1, 2, 3
        v_and_b32_dpp v[.LPA+2*\x], v[.LTT+\x], v[.LMKX] quad_perm:[\t,\t,\t,\t] row_mask:0xf bank_mask:0xf
        v_and_b32_dpp v[.LPA+2*\x+1], v[.LTT+\x], v[.LMKY] quad_perm:[\t,\t,\t,\t] row_mask:0xf bank_mask:0xf
      .endr
      .if \last == 0
        GATE
      .endif
      .irp x, 0, 1, 2, 3
        .if \last
          s_waitcnt vmcnt(31-16*\g-4*\t-\x)
        .endif
        v_mfma_f32_16x16x16_bf16 v[.LA0:.LA0+3], v[.LPA+2*\x:.LPA+2*\x+1], v[.LRG+64*\g+16*\t+4*\x:.LRG+64*\g+16*\t+4*\x+1], v[.LA0:.LA0+3]
        v_mfma_f32_16x16x16_bf16 v[.LA1:.LA1+3], v[.LPA+2*\x:.LPA+2*\x+1], v[.LRG+64*\g+16*\t+4*\x+2:.LRG+64*\g+16*\t+4*\x+3], v[.LA1:.LA1+3]
        .if \last == 0
          LOAD 16*\g+4*\t+\x
        .endif
      .endr
    .endm
    .macro VGROUP g, last
      VTILE \g, 0, \last
      VTILE \g, 1, \last
      VTILE \g, 2, \last
      VTILE \g, 3, \last
    .endm
    .macro RECIP                  ; RL = 1 / l of query lane>>4
      v_add_f32_dpp v[.LRL], v[.LLL], v[.LLL] quad_perm:[1,0,3,2] row_mask:0xf bank_mask:0xf
      s_nop 1
      v_add_f32_dpp v[.LRL], v[.LRL], v[.LRL] quad_perm:[2,3,0,1] row_mask:0xf bank_mask:0xf
      XROWS v_add_f32, .LRL
      v_rcp_f32 v[.LT1], v[.LRL]
      s_nop 2
      .irp x, 0, 1, 2, 3
        v_mov_b32_dpp v[.LRL], v[.LT1] row_newbcast:4*\x row_mask:1<<\x bank_mask:0xf
        s_nop 1
      .endr
    .endm
    .macro NORM                   ; TT = bf16(O / l)
      .irp x, 0, 1, 2, 3
        v_mul_f32 v[.LA0+\x], v[.LRL], v[.LA0+\x]
        v_mul_f32 v[.LA1+\x], v[.LRL], v[.LA1+\x]
      .endr
      .irp x, 0, 1, 2, 3
        v_cvt_pk_bf16_f32 v[.LTT+\x], v[.LA0+2*\x], v[.LA0+2*\x+1]
      .endr
    .endm

    ; ================= prologue: first loads as early as possible
    s_load_dwordx8 s[4:11], %[ka], 0x0
    s_load_dwordx4 s[12:15], %[ka], 0x20
    v_readfirstlane_b32 s21, %[tid]
    v_and_b32 v[.LT0], 56, %[tid]
    v_and_b32 v[.LT1], 7, %[tid]
    v_lshlrev_b32 v[.LT0], 1, v[.LT0]                      ; 16-B chunk (lane >> 3) of the line
    s_lshl_b32 s16, %[bx], 21
    v_lshl_or_b32 v[.LKO], v[.LT1], 8, v[.LT0]             ; key lane & 7
    s_cmp_ge_u32 s21, 64
    s_cbranch_scc1 .Lpace%=
    s_waitcnt lgkmcnt(0)
    LOAD0 0
    v_and_b32 v[.LT1], 3, %[tid]
    s_lshl_b32 s21, %[bx], 11
    v_lshl_or_b32 v[.LT0], v[.LT1], 8, v[.LT0]             ; query lane & 3
    buffer_load_dwordx4 v[.LQR:.LQR+3], v[.LT0], s[8:11], s21 offen
    buffer_load_dwordx4 v[.LQR+4:.LQR+7], v[.LT0], s[8:11], s21 offen offset:128
    buffer_load_dwordx4 v[.LQR+8:.LQR+11], v[.LT0], s[8:11], s21 offen offset:1024
    buffer_load_dwordx4 v[.LQR+12:.LQR+15], v[.LT0], s[8:11], s21 offen offset:1152
    .irp t, 1, 2, 3, 4, 5, 6, 7
      LOAD0 \t
    .endr
    s_add_u32 s14, s14, s21
    s_addc_u32 s15, s15, 0
    s_mov_b32 s20, 0x3e0293ee
    v_add_u32 v[.LKO+1], 128, v[.LKO]
    v_add_u32 v[.LKO+2], 2048, v[.LKO]
    v_add_u32 v[.LKO+3], 2176, v[.LKO]
    v_lshlrev_b32 v[.LOO], 4, %[tid]
    v_and_b32 v[.LT0], 48, %[tid]
    v_add_u32 v[.LLR], %[ldsp], v[.LOO]
    v_lshlrev_b32 v[.LLW], 6, v[.LT1]                      ; LDS slot 16r + 4q + 2*pair + tile
    v_lshl_or_b32 v[.LLW], v[.LT0], 4, v[.LLW]
    v_and_b32 v[.LT0], 12, %[tid]
    v_lshl_or_b32 v[.LLW], v[.LT0], 2, v[.LLW]
    v_add_u32 v[.LLW], %[ldsp], v[.LLW]
    v_and_b32 v[.LT0], 4, %[tid]
    v_cmp_eq_u32 s[24:25], 0, v[.LT0]
    v_and_b32 v[.LT0], 1, %[tid]
    v_lshlrev_b32 v[.LT0], 4, v[.LT0]
    v_mov_b32 v[.LMX], 0xffff
    v_lshlrev_b32 v[.LT0], v[.LT0], v[.LMX]                ; element j0 = lane & 3 of the 4-element A fragment
    v_cmp_gt_u32 vcc, 2, v[.LT1]
    v_mov_b32 v[.LPG], 0
    v_mov_b32 v[.LMD], 1
    v_cndmask_b32 v[.LMKX], 0, v[.LT0], vcc
    v_cndmask_b32 v[.LMKY], v[.LT0], 0, vcc
    s_add_u32 s21, %[ldsp], %[ctl]
    v_mov_b32 v[.LCA], s21
    ds_write2_b32 v[.LCA], v[.LPG], v[.LMD] offset1:2
    s_memrealtime s[22:23]
    s_waitcnt lgkmcnt(0)
    s_barrier
    s_mov_b32 s17, 32
    s_mov_b32 s18, 0
    s_waitcnt vmcnt(28)

    ; ================= K phase: 32 rounds per head
  .LKhead%=:
    v_mov_b32 v[.LMX], 0xff7fffff
    .irp x, 0, 1, 2, 3
      v_cndmask_b32 v[.LBQ+\x], 0, v[.LQR+\x], s[24:25]
      v_cndmask_b32 v[.LBQ+4+\x], v[.LQR+\x], 0, s[24:25]
    .endr
    .irp x, 0, 1, 2, 3
      v_cndmask_b32 v[.LBQ+8+\x], 0, v[.LQR+4+\x], s[24:25]
      v_cndmask_b32 v[.LBQ+12+\x], v[.LQR+4+\x], 0, s[24:25]
    .endr
  .LK%=:
    ROUND
    s_cmp_eq_u32 s19, 0
    s_cselect_b64 vcc, -1, 0
    KGROUP 0
    MID
    KGROUP 1
    v_add_u32 v[.LLW], 0x800, v[.LLW]
    s_sub_u32 s17, s17, 1
    s_cmp_lg_u32 s17, 0
    s_cbranch_scc1 .LK%=
    s_cmp_eq_u32 s18, 1
    s_cbranch_scc0 .LKmax%=
    ; the last K round refills the ring with the first V tiles: load x of a tile = keys 4r + x
    s_lshl_b32 s21, %[bx], 21
    s_add_u32 s4, s12, s21
    s_addc_u32 s5, s13, 0
    s_mov_b32 s6, 0x200000
    s_mov_b32 s16, 0
    v_and_b32 v[.LT0], 48, %[tid]
    v_and_b32 v[.LKO], 0xf0, v[.LOO]
    v_lshl_or_b32 v[.LKO], v[.LT0], 6, v[.LKO]
    v_add_u32 v[.LKO+1], 256, v[.LKO]
    v_add_u32 v[.LKO+2], 512, v[.LKO]
    v_add_u32 v[.LKO+3], 768, v[.LKO]
    s_mov_b32 s18, 2
    s_mov_b32 s17, 1
    s_branch .LK%=
  .LKmax%=:                                                 ; row maxima of the finished head (query = lane & 3) -> M1 (query = (lane >> 2) & 3)
    v_mov_b32 v[.LM0], v[.LM1]
    v_max_f32_dpp v[.LMX], v[.LMX], v[.LMX] row_ror:4 row_mask:0xf bank_mask:0xf
    s_nop 1
    v_max_f32_dpp v[.LMX], v[.LMX], v[.LMX] row_ror:8 row_mask:0xf bank_mask:0xf
    XROWS v_max_f32, .LMX
    v_mul_f32 v[.LMX], s20, v[.LMX]
    s_nop 1
    .irp x, 0, 1, 2, 3
      v_mov_b32_dpp v[.LM1], v[.LMX] row_newbcast:\x row_mask:0xf bank_mask:1<<\x
      s_nop 1
    .endr
    s_cmp_eq_u32 s18, 0
    s_cbranch_scc0 .LKdone%=
    .irp x, 0, 1, 2, 3, 4, 5, 6, 7
      v_mov_b32 v[.LQR+\x], v[.LQR+8+\x]
    .endr
    s_mov_b32 s18, 1
    s_mov_b32 s17, 31
    s_branch .LKhead%=

    ; ================= V phase: 32 rounds per head, the very last one peeled
  .LKdone%=:
    ds_read_b128 v[.LSC:.LSC+3], v[.LLR]
    s_mov_b32 s18, 63
    s_mov_b32 s17, 32
  .LVhead%=:
    v_mov_b32 v[.LLL], 0
    .irp x, 0, 1, 2, 3
      v_mov_b32 v[.LA0+\x], 0
      v_mov_b32 v[.LA1+\x], 0
    .endr
  .LV%=:
    ROUND
    s_cmp_le_u32 s18, 1
    s_cselect_b32 s19, 0, s19                               ; the round that issues the last loads is not throttled
    s_sub_u32 s18, s18, 1
    s_cmp_eq_u32 s19, 0
    s_cselect_b64 vcc, -1, 0
    ds_read_b128 v[.LSC+4:.LSC+7], v[.LLR] offset:1024
    PGROUP 0
    VGROUP 0, 0
    s_waitcnt lgkmcnt(0)
    ds_read_b128 v[.LSC:.LSC+3], v[.LLR] offset:2048
    MID
    PGROUP 1
    VGROUP 1, 0
    v_add_u32 v[.LLR], 0x800, v[.LLR]
    s_sub_u32 s17, s17, 1
    s_cmp_lg_u32 s17, 0
    s_cbranch_scc1 .LV%=
    s_cmp_eq_u32 s18, 0
    s_cbranch_scc1 .LVlast%=
    RECIP
    NORM
    .irp x, 0, 1, 2, 3
      v_mov_b32 v[.LO0+\x], v[.LTT+\x]
    .endr
    v_mov_b32 v[.LM0], v[.LM1]
    s_mov_b32 s17, 31
    s_branch .LVhead%=
  .LVlast%=:
    s_waitcnt lgkmcnt(0)
    ds_read_b128 v[.LSC+4:.LSC+7], v[.LLR] offset:1024
    PGROUP 0
    VGROUP 0, 1
    s_waitcnt lgkmcnt(0)
    PGROUP 1
    s_nop 1
    RECIP
    VGROUP 1, 1
    global_store_dwordx4 v[.LOO], v[.LO0:.LO0+3], s[14:15]
    s_nop 6
    NORM
    global_store_dwordx4 v[.LOO], v[.LTT:.LTT+3], s[14:15] offset:1024
    s_waitcnt vmcnt(0)
    s_branch .Lend%=

    ; ================= pacing wave: pull the streaming wave's code into L2 (one dword per 128-B line of .LKhead .. .Lpace)
  .Lpace%=:
    s_waitcnt lgkmcnt(0)
    s_barrier
    s_getpc_b64 s[4:5]
  .Lpc%=:
    s_sub_u32 s4, s4, .Lpc%=-.LKhead%=
    s_subb_u32 s5, s5, 0
    v_and_b32 v[.LT0], 63, %[tid]
    v_lshlrev_b32 v[.LT0], 7, v[.LT0]
    v_min_u32 v[.LT0], .Lpace%=-.LKhead%=, v[.LT0]
    global_load_dword v[.LT1], v[.LT0], s[4:5]
    s_waitcnt vmcnt(0)
  .Lend%=:
    )ASM"
      :
      : [ka] "s"(__builtin_amdgcn_kernarg_segment_ptr()), [bx] "s"(blockIdx.x), [tid] "v"(threadIdx.x), [ldsp] "s"((LAS float*)lds), [ctl] "n"(SCORE_WORDS * 4)
      : "memory", "vcc", "scc",
#define V8(a, b, c, d, e, f, g, h) "v" #a, "v" #b, "v" #c, "v" #d, "v" #e, "v" #f, "v" #g, "v" #h
#define V10(t) "v" #t "0", "v" #t "1", "v" #t "2", "v" #t "3", "v" #t "4", "v" #t "5", "v" #t "6", "v" #t "7", "v" #t "8", "v" #t "9"
        V8(1, 2, 3, 4, 5, 6, 7, 8), "v9", V10(1), V10(2), V10(3), V10(4), V10(5), V10(6), V10(7), V10(8), V10(9), V10(10), V10(11), V10(12), V10(13), V10(14), V10(15),
        V10(16), V10(17), V10(18), V10(19), V10(20), V10(21), V10(22), "v230", "v231", "v232", "v233",
        "s4", "s5", "s6", "s7", "s8", "s9", "s10", "s11", "s12", "s13", "s14", "s15", "s16", "s17", "s18", "s19", "s20", "s21", "s22", "s23", "s24", "s25");
  if (__builtin_amdgcn_readfirstlane(threadIdx.x) < 64) return;

  const int lane = threadIdx.x & 63, bx = blockIdx.x;
  volatile LAS int* ctl = (volatile LAS int*)(lds + SCORE_WORDS);
  const auto rs = __builtin_amdgcn_make_buffer_rsrc((void*)ws, 0, 2 * NPUB * 128, 0x00020000);
  int next = MS0, m0 = 0, t0 = 0, t1 = 0, t2 = 0;
  int want = STEP, read_at = STEP + DLY + ((bx >> 3) & 7);
  int T = 0, P = -1;
  float tau = 0.f;
  for (;;) {
    __builtin_amdgcn_s_sleep(8);
    const unsigned long long pt = *(volatile LAS unsigned long long*)ctl;
    const int p = __builtin_amdgcn_readfirstlane((int)pt), t = __builtin_amdgcn_readfirstlane((int)(pt >> 32));
    if (p >= PEND) break;
    if (p >= next) {
      t2 = t1; t1 = t0; t0 = t - (p - next) * TAU0; m0 = next; next = (next / STEP + 1) * STEP;
      if (bx < NPUB && m0 >= STEP && lane == 0)
        __builtin_amdgcn_raw_buffer_store_b128(u32x4{(unsigned)m0, (unsigned)t1, (unsigned)t0, 0u}, rs, (((m0 / STEP) & 1) * NPUB + bx) * 128, 0, 16  );
    }
    if (p >= read_at) {
      const int tc = m0 == want ? t0 : t1, tp = m0 == want ? t1 : t2;
      const u32x4 m = __builtin_amdgcn_raw_buffer_load_b128(rs, lane < NPUB ? (((want / STEP) & 1) * NPUB + lane) * 128 : 0x10000, 0, 16  );
      const int d0 = (int)m[1] - tp, d1 = (int)m[2] - tc;
      const bool ok = (int)m[0] == want && d0 > -NEAR && d0 < NEAR && d1 > -NEAR && d1 < NEAR;
      const int s0 = sum16(ok ? d0 : 0), s1 = sum16(ok ? d1 : 0), n = sum16(ok ? 1 : 0);
      if (n > 0 && (m0 == want || m0 == want + STEP)) {
        const float inv = 1.f / (float)n, a0 = (float)s0 * inv, a1 = (float)s1 * inv;
        const float tn = ((float)(tc - tp) + a1 - a0) * (want == STEP ? 1.f / (STEP - MS0) : 1.f / STEP);
        tau = P < 0 ? tn : tau + FILT * fminf(fmaxf(tn - tau, -CLMP), CLMP);
        T = tc + (int)a1; P = want;
      }
      want += STEP; read_at += STEP;
    }
    if (P >= 0) {
      const int late = t - T - (int)((float)(p - P) * tau);
      ctl[2] = late > TH ? 0 : late < -TH ? 2 : 1;
    }
  }
}
}

extern "C" void kernel_launch(void* const* d_in, const int* in_sizes, int n_in, void* d_out, int out_size, void* d_ws, size_t ws_size, hipStream_t stream) {
  attn_k<<<NBH / HPW, 128, 0, stream>>>((const char*)d_in[1], (unsigned)NBH * SKV * D * 2, 0x00020000u, (const char*)d_in[0], (unsigned)NBH * SQ * D * 2, 0x00020000u,
                                        (const char*)d_in[2], (char*)d_out, (char*)d_ws);
}
```

```cpp
#include <hip/hip_runtime.h>
#include <cstdint>

namespace {
constexpr int D = 128, SKV = 4096, SQ = 4, NBH = 512, HPW = 2;
constexpr int SCORE_WORDS = HPW * SKV * SQ;
constexpr int NPUB = 8, MS0 = 4, STEP = 12, DLY = 4, PEND = 126, TAU0 = 110, TH = 15, NEAR = 2000;
constexpr float FILT = 0.7f, CLMP = 3.f;
using u32x4 = __attribute__((ext_vector_type(4))) unsigned;
#define LAS __attribute__((address_space(3)))
template <int CTRL> __device__ __forceinline__ int dpp(int x) { return __builtin_amdgcn_mov_dpp(x, CTRL, 0xf, 0xf, true); }
__device__ __forceinline__ int sum16(int x) { x += dpp<0xB1>(x); x += dpp<0x4E>(x); x += dpp<0x141>(x); x += dpp<0x128>(x); return __builtin_amdgcn_readfirstlane(x); }

__global__ void __launch_bounds__(128, 1) attn_k(const char* k, unsigned kn, unsigned kc, const char* q, unsigned qn, unsigned qc, const char* v, char* out, char* ws) {
  __shared__ __attribute__((aligned(16))) float lds[SCORE_WORDS + 4];
  asm volatile(R"ASM(
    ; ---- vector registers
    .set .LT0, 1      ; temporaries
    .set .LT1, 2
    .set .LOO, 3      ; lane * 16
    .set .LKO, 4      ; 4 per-lane offsets of a tile's loads
    .set .LRG, 8      ; ring: 32 loads x 4
    .set .LQR, 136    ; Q fragments [head][line] x 4
    .set .LBQ, 152    ; B operands [line][tile of the pair] x 4
    .set .LDA, 168    ; scores of keys 0..7  [pair] x 4
    .set .LDB, 176    ; scores of keys 8..15 [pair] x 4
    .set .LRR, 184    ; scores of 4 tiles
    .set .LSC, 188    ; scores read back [2] x 4
    .set .LTT, 196    ; packed p
    .set .LPA, 200    ; A operands of P V, 4 x 2
    .set .LA0, 208    ; O[q][8c + 0..3]
    .set .LA1, 212    ; O[q][8c + 4..7]
    .set .LO0, 216    ; packed output of head 0
    .set .LPG, 220    ; {round, time}
    .set .LMX, 222
    .set .LM0, 223    ; max * c of the head in work
    .set .LM1, 224    ; max * c of head 1
    .set .LLL, 226    ; l
    .set .LRL, 227    ; 1 / l
    .set .LLW, 228    ; LDS write address
    .set .LLR, 229    ; LDS read address
    .set .LMKX, 230
    .set .LMKY, 231
    .set .LMD, 232    ; pacing mode
    .set .LCA, 233    ; LDS address of {round, time, mode}
    ; ---- scalar registers: s[4:7] K (later V) descriptor, s[8:11] Q descriptor, s[12:13] v, s[14:15] out, s16 tile offset, s17 rounds left of this head,
    ;      s18 phase / rounds left, s19 mode, s20 c = log2(e)/sqrt(D), s21 temporary, s[22:23] clock, s[24:25] lanes of tile 0 of a pair

    .macro LOAD m, off            ; ring slot m <- 1 KiB at tile offset s16
      buffer_load_dwordx4 v[.LRG+4*(\m):.LRG+4*(\m)+3], v[.LKO+((\m)&3)], s[4:7], s16 offen nt
      .if ((\m)&3) == 3
        s_addk_i32 s16, 0x1000
      .endif
    .endm
    .macro LOAD0 t                ; first K tiles, before the four offsets exist
      buffer_load_dwordx4 v[.LRG+16*(\t):.LRG+16*(\t)+3], v[.LKO], s[4:7], s16 offen nt
      buffer_load_dwordx4 v[.LRG+16*(\t)+4:.LRG+16*(\t)+7], v[.LKO], s[4:7], s16 offen offset:128 nt
      buffer_load_dwordx4 v[.LRG+16*(\t)+8:.LRG+16*(\t)+11], v[.LKO], s[4:7], s16 offen offset:2048 nt
      buffer_load_dwordx4 v[.LRG+16*(\t)+12:.LRG+16*(\t)+15], v[.LKO], s[4:7], s16 offen offset:2176 nt
      s_addk_i32 s16, 0x1000
    .endm
    .macro GATE                   ; in front of a tile: its 4 loads have landed; pacing: at most 28 / 20 / 17 loads in flight (mode 0 / 1 / 2) before 4 more go out
      s_cbranch_vccnz 1f
      s_waitcnt vmcnt(20)
      s_cmp_eq_u32 s19, 1
      s_cbranch_scc1 1f
      s_waitcnt vmcnt(17)
    1:
      s_waitcnt vmcnt(28)
    .endm
    .macro ROUND                  ; start of a round: report {round, time}, take the mode
      s_waitcnt lgkmcnt(0)
      v_readfirstlane_b32 s19, v[.LMD]
      v_add_u32 v[.LPG], 1, v[.LPG]
      v_mov_b32 v[.LPG+1], s22
      ds_write_b64 v[.LCA], v[.LPG:.LPG+1]
    .endm
    .macro MID
      s_memrealtime s[22:23]
      ds_read_b32 v[.LMD], v[.LCA] offset:8
    .endm
    .macro XROWS op, r            ; r = op over the four 16-lane rows
      v_mov_b32 v[.LT1], v[\r]
      s_nop 1
      v_permlane16_swap_b32 v[\r], v[.LT1]
      s_nop 1
      \op v[\r], v[\r], v[.LT1]
      v_mov_b32 v[.LT1], v[\r]
      s_nop 1
      v_permlane32_swap_b32 v[\r], v[.LT1]
      s_nop 1
      \op v[\r], v[\r], v[.LT1]
    .endm
    .macro KTILE m, pair, tp      ; 16 keys: ring slots m..m+3
      GATE
      .if \tp == 0
        v_mfma_f32_16x16x32_bf16 v[.LDA+4*\pair:.LDA+4*\pair+3], v[.LRG+4*(\m):.LRG+4*(\m)+3], v[.LBQ:.LBQ+3], 0
      .else
        v_mfma_f32_16x16x32_bf16 v[.LDA+4*\pair:.LDA+4*\pair+3], v[.LRG+4*(\m):.LRG+4*(\m)+3], v[.LBQ+4:.LBQ+7], v[.LDA+4*\pair:.LDA+4*\pair+3]
      .endif
      LOAD \m
      v_mfma_f32_16x16x32_bf16 v[.LDA+4*\pair:.LDA+4*\pair+3], v[.LRG+4*(\m)+4:.LRG+4*(\m)+7], v[.LBQ+8+4*\tp:.LBQ+11+4*\tp], v[.LDA+4*\pair:.LDA+4*\pair+3]
      LOAD \m+1
      .if \tp == 0
        v_mfma_f32_16x16x32_bf16 v[.LDB+4*\pair:.LDB+4*\pair+3], v[.LRG+4*(\m)+8:.LRG+4*(\m)+11], v[.LBQ:.LBQ+3], 0
      .else
        v_mfma_f32_16x16x32_bf16 v[.LDB+4*\pair:.LDB+4*\pair+3], v[.LRG+4*(\m)+8:.LRG+4*(\m)+11], v[.LBQ+4:.LBQ+7], v[.LDB+4*\pair:.LDB+4*\pair+3]
      .endif
      LOAD \m+2
      v_mfma_f32_16x16x32_bf16 v[.LDB+4*\pair:.LDB+4*\pair+3], v[.LRG+4*(\m)+12:.LRG+4*(\m)+15], v[.LBQ+8+4*\tp:.LBQ+11+4*\tp], v[.LDB+4*\pair:.LDB+4*\pair+3]
      LOAD \m+3
    .endm
    .macro KPAIR m, pair          ; two tiles -> columns 8*pair..8*pair+7 of RR
      KTILE \m, \pair, 0
      KTILE \m+4, \pair, 1
      s_nop 5
      .irp x, 0, 1, 2, 3
        v_permlane32_swap_b32 v[.LDA+4*\pair+\x], v[.LDB+4*\pair+\x]
      .endr
      s_nop 1
      .irp x, 0, 1, 2, 3
        .if \pair == 0
          v_add_f32_dpp v[.LRR+\x], v[.LDB+\x], v[.LDA+\x] row_ror:8 row_mask:0xf bank_mask:0x3
        .else
          v_add_f32_dpp v[.LRR+\x], v[.LDA+4+\x], v[.LDB+4+\x] row_ror:8 row_mask:0xf bank_mask:0xc
        .endif
      .endr
    .endm
    .macro KGROUP g               ; 64 keys: lane (row r, column 8*pair + 4*tile + q), register x = score of key 4r+x
      KPAIR 16*\g, 0
      KPAIR 16*\g+8, 1
      ds_write_b128 v[.LLW], v[.LRR:.LRR+3] offset:1024*\g
      v_max3_f32 v[.LMX], v[.LMX], v[.LRR], v[.LRR+1]
      v_max3_f32 v[.LMX], v[.LMX], v[.LRR+2], v[.LRR+3]
    .endm
    .macro PGROUP g               ; scores of 64 keys, lane (row r, column 4q + tile) -> packed p, l
      .irp x, 0, 1, 2, 3
        v_fma_f32 v[.LSC+4*\g+\x], v[.LSC+4*\g+\x], s20, -v[.LM0]
      .endr
      .irp x, 0, 1, 2, 3
        v_exp_f32 v[.LSC+4*\g+\x], v[.LSC+4*\g+\x]
      .endr
      .irp x, 0, 1, 2, 3
        v_cvt_pk_bf16_f32 v[.LTT+\x], v[.LSC+4*\g+\x], v[.LSC+4*\g+\x]
      .endr
      v_add_f32 v[.LSC+4*\g], v[.LSC+4*\g], v[.LSC+4*\g+1]
      v_add_f32 v[.LSC+4*\g+2], v[.LSC+4*\g+2], v[.LSC+4*\g+3]
      v_add_f32 v[.LLL], v[.LLL], v[.LSC+4*\g]
      v_add_f32 v[.LLL], v[.LLL], v[.LSC+4*\g+2]
    .endm
    .macro VTILE g, t, last       ; 16 keys: ring slots 16g+4t .. +3; the last round only drains the ring
      .irp x, 0, 1, 2, 3
        v_and_b32_dpp v[.LPA+2*\x], v[.LTT+\x], v[.LMKX] quad_perm:[\t,\t,\t,\t] row_mask:0xf bank_mask:0xf
        v_and_b32_dpp v[.LPA+2*\x+1], v[.LTT+\x], v[.LMKY] quad_perm:[\t,\t,\t,\t] row_mask:0xf bank_mask:0xf
      .endr
      .if \last == 0
        GATE
      .endif
      .irp x, 0, 1, 2, 3
        .if \last
          s_waitcnt vmcnt(31-16*\g-4*\t-\x)
        .endif
        v_mfma_f32_16x16x16_bf16 v[.LA0:.LA0+3], v[.LPA+2*\x:.LPA+2*\x+1], v[.LRG+64*\g+16*\t+4*\x:.LRG+64*\g+16*\t+4*\x+1], v[.LA0:.LA0+3]
        v_mfma_f32_16x16x16_bf16 v[.LA1:.LA1+3], v[.LPA+2*\x:.LPA+2*\x+1], v[.LRG+64*\g+16*\t+4*\x+2:.LRG+64*\g+16*\t+4*\x+3], v[.LA1:.LA1+3]
        .if \last == 0
          LOAD 16*\g+4*\t+\x
        .endif
      .endr
    .endm
    .macro VGROUP g, last
      VTILE \g, 0, \last
      VTILE \g, 1, \last
      VTILE \g, 2, \last
      VTILE \g, 3, \last
    .endm
    .macro RECIP                  ; RL = 1 / l of query lane>>4
      v_add_f32_dpp v[.LRL], v[.LLL], v[.LLL] quad_perm:[1,0,3,2] row_mask:0xf bank_mask:0xf
      s_nop 1
      v_add_f32_dpp v[.LRL], v[.LRL], v[.LRL] quad_perm:[2,3,0,1] row_mask:0xf bank_mask:0xf
      XROWS v_add_f32, .LRL
      v_rcp_f32 v[.LT1], v[.LRL]
      s_nop 2
      .irp x, 0, 1, 2, 3
        v_mov_b32_dpp v[.LRL], v[.LT1] row_newbcast:4*\x row_mask:1<<\x bank_mask:0xf
        s_nop 1
      .endr
    .endm
    .macro NORM                   ; TT = bf16(O / l)
      .irp x, 0, 1, 2, 3
        v_mul_f32 v[.LA0+\x], v[.LRL], v[.LA0+\x]
        v_mul_f32 v[.LA1+\x], v[.LRL], v[.LA1+\x]
      .endr
      .irp x, 0, 1, 2, 3
        v_cvt_pk_bf16_f32 v[.LTT+\x], v[.LA0+2*\x], v[.LA0+2*\x+1]
      .endr
    .endm

    ; ================= prologue: first loads as early as possible
    s_load_dwordx8 s[4:11], %[ka], 0x0
    s_load_dwordx4 s[12:15], %[ka], 0x20
    v_readfirstlane_b32 s21, %[tid]
    v_and_b32 v[.LT0], 56, %[tid]
    v_and_b32 v[.LT1], 7, %[tid]
    v_lshlrev_b32 v[.LT0], 1, v[.LT0]                      ; 16-B chunk (lane >> 3) of the line
    s_lshl_b32 s16, %[bx], 21
    v_lshl_or_b32 v[.LKO], v[.LT1], 8, v[.LT0]             ; key lane & 7
    s_cmp_ge_u32 s21, 64
    s_cbranch_scc1 .Lpace%=
    s_waitcnt lgkmcnt(0)
    LOAD0 0
    v_and_b32 v[.LT1], 3, %[tid]
    s_lshl_b32 s21, %[bx], 11
    v_lshl_or_b32 v[.LT0], v[.LT1], 8, v[.LT0]             ; query lane & 3
    buffer_load_dwordx4 v[.LQR:.LQR+3], v[.LT0], s[8:11], s21 offen
    buffer_load_dwordx4 v[.LQR+4:.LQR+7], v[.LT0], s[8:11], s21 offen offset:128
    buffer_load_dwordx4 v[.LQR+8:.LQR+11], v[.LT0], s[8:11], s21 offen offset:1024
    buffer_load_dwordx4 v[.LQR+12:.LQR+15], v[.LT0], s[8:11], s21 offen offset:1152
    .irp t, 1, 2, 3, 4, 5, 6, 7
      LOAD0 \t
    .endr
    s_add_u32 s14, s14, s21
    s_addc_u32 s15, s15, 0
    s_mov_b32 s20, 0x3e0293ee
    v_add_u32 v[.LKO+1], 128, v[.LKO]
    v_add_u32 v[.LKO+2], 2048, v[.LKO]
    v_add_u32 v[.LKO+3], 2176, v[.LKO]
    v_lshlrev_b32 v[.LOO], 4, %[tid]
    v_and_b32 v[.LT0], 48, %[tid]
    v_add_u32 v[.LLR], %[ldsp], v[.LOO]
    v_lshlrev_b32 v[.LLW], 6, v[.LT1]                      ; LDS slot 16r + 4q + 2*pair + tile
    v_lshl_or_b32 v[.LLW], v[.LT0], 4, v[.LLW]
    v_and_b32 v[.LT0], 12, %[tid]
    v_lshl_or_b32 v[.LLW], v[.LT0], 2, v[.LLW]
    v_add_u32 v[.LLW], %[ldsp], v[.LLW]
    v_and_b32 v[.LT0], 4, %[tid]
    v_cmp_eq_u32 s[24:25], 0, v[.LT0]
    v_and_b32 v[.LT0], 1, %[tid]
    v_lshlrev_b32 v[.LT0], 4, v[.LT0]
    v_mov_b32 v[.LMX], 0xffff
    v_lshlrev_b32 v[.LT0], v[.LT0], v[.LMX]                ; element j0 = lane & 3 of the 4-element A fragment
    v_cmp_gt_u32 vcc, 2, v[.LT1]
    v_mov_b32 v[.LPG], 0
    v_mov_b32 v[.LMD], 1
    v_cndmask_b32 v[.LMKX], 0, v[.LT0], vcc
    v_cndmask_b32 v[.LMKY], v[.LT0], 0, vcc
    s_add_u32 s21, %[ldsp], %[ctl]
    v_mov_b32 v[.LCA], s21
    ds_write2_b32 v[.LCA], v[.LPG], v[.LMD] offset1:2
    s_memrealtime s[22:23]
    s_waitcnt lgkmcnt(0)
    s_barrier
    s_mov_b32 s17, 32
    s_mov_b32 s18, 0
    s_waitcnt vmcnt(28)

    ; ================= K phase: 32 rounds per head
  .LKhead%=:
    v_mov_b32 v[.LMX], 0xff7fffff
    .irp x, 0, 1, 2, 3
      v_cndmask_b32 v[.LBQ+\x], 0, v[.LQR+\x], s[24:25]
      v_cndmask_b32 v[.LBQ+4+\x], v[.LQR+\x], 0, s[24:25]
    .endr
    .irp x, 0, 1, 2, 3
      v_cndmask_b32 v[.LBQ+8+\x], 0, v[.LQR+4+\x], s[24:25]
      v_cndmask_b32 v[.LBQ+12+\x], v[.LQR+4+\x], 0, s[24:25]
    .endr
  .LK%=:
    ROUND
    s_cmp_eq_u32 s19, 0
    s_cselect_b64 vcc, -1, 0
    KGROUP 0
    MID
    KGROUP 1
    v_add_u32 v[.LLW], 0x800, v[.LLW]
    s_sub_u32 s17, s17, 1
    s_cmp_lg_u32 s17, 0
    s_cbranch_scc1 .LK%=
    s_cmp_eq_u32 s18, 1
    s_cbranch_scc0 .LKmax%=
    ; the last K round refills the ring with the first V tiles: load x of a tile = keys 4r + x
    s_lshl_b32 s21, %[bx], 21
    s_add_u32 s4, s12, s21
    s_addc_u32 s5, s13, 0
    s_mov_b32 s6, 0x200000
    s_mov_b32 s16, 0
    v_and_b32 v[.LT0], 48, %[tid]
    v_and_b32 v[.LKO], 0xf0, v[.LOO]
    v_lshl_or_b32 v[.LKO], v[.LT0], 6, v[.LKO]
    v_add_u32 v[.LKO+1], 256, v[.LKO]
    v_add_u32 v[.LKO+2], 512, v[.LKO]
    v_add_u32 v[.LKO+3], 768, v[.LKO]
    s_mov_b32 s18, 2
    s_mov_b32 s17, 1
    s_branch .LK%=
  .LKmax%=:                                                 ; row maxima of the finished head (query = lane & 3) -> M1 (query = (lane >> 2) & 3)
    v_mov_b32 v[.LM0], v[.LM1]
    v_max_f32_dpp v[.LMX], v[.LMX], v[.LMX] row_ror:4 row_mask:0xf bank_mask:0xf
    s_nop 1
    v_max_f32_dpp v[.LMX], v[.LMX], v[.LMX] row_ror:8 row_mask:0xf bank_mask:0xf
    XROWS v_max_f32, .LMX
    v_mul_f32 v[.LMX], s20, v[.LMX]
    s_nop 1
    .irp x, 0, 1, 2, 3
      v_mov_b32_dpp v[.LM1], v[.LMX] row_newbcast:\x row_mask:0xf bank_mask:1<<\x
      s_nop 1
    .endr
    s_cmp_eq_u32 s18, 0
    s_cbranch_scc0 .LKdone%=
    .irp x, 0, 1, 2, 3, 4, 5, 6, 7
      v_mov_b32 v[.LQR+\x], v[.LQR+8+\x]
    .endr
    s_mov_b32 s18, 1
    s_mov_b32 s17, 31
    s_branch .LKhead%=

    ; ================= V phase: 32 rounds per head, the very last one peeled
  .LKdone%=:
    ds_read_b128 v[.LSC:.LSC+3], v[.LLR]
    s_mov_b32 s18, 63
    s_mov_b32 s17, 32
  .LVhead%=:
    v_mov_b32 v[.LLL], 0
    .irp x, 0, 1, 2, 3
      v_mov_b32 v[.LA0+\x], 0
      v_mov_b32 v[.LA1+\x], 0
    .endr
  .LV%=:
    ROUND
    s_cmp_le_u32 s18, 1
    s_cselect_b32 s19, 0, s19                               ; the round that issues the last loads is not throttled
    s_sub_u32 s18, s18, 1
    s_cmp_eq_u32 s19, 0
    s_cselect_b64 vcc, -1, 0
    ds_read_b128 v[.LSC+4:.LSC+7], v[.LLR] offset:1024
    PGROUP 0
    VGROUP 0, 0
    s_waitcnt lgkmcnt(0)
    ds_read_b128 v[.LSC:.LSC+3], v[.LLR] offset:2048
    MID
    PGROUP 1
    VGROUP 1, 0
    v_add_u32 v[.LLR], 0x800, v[.LLR]
    s_sub_u32 s17, s17, 1
    s_cmp_lg_u32 s17, 0
    s_cbranch_scc1 .LV%=
    s_cmp_eq_u32 s18, 0
    s_cbranch_scc1 .LVlast%=
    RECIP
    NORM
    .irp x, 0, 1, 2, 3
      v_mov_b32 v[.LO0+\x], v[.LTT+\x]
    .endr
    v_mov_b32 v[.LM0], v[.LM1]
    s_mov_b32 s17, 31
    s_branch .LVhead%=
  .LVlast%=:
    s_waitcnt lgkmcnt(0)
    ds_read_b128 v[.LSC+4:.LSC+7], v[.LLR] offset:1024
    PGROUP 0
    VGROUP 0, 1
    s_waitcnt lgkmcnt(0)
    PGROUP 1
    s_nop 1
    RECIP
    VGROUP 1, 1
    global_store_dwordx4 v[.LOO], v[.LO0:.LO0+3], s[14:15]
    s_nop 6
    NORM
    global_store_dwordx4 v[.LOO], v[.LTT:.LTT+3], s[14:15] offset:1024
    s_waitcnt vmcnt(0)
    s_branch .Lend%=

    ; ================= pacing wave: pull the streaming wave's code into L2 (one dword per 128-B line of .LKhead .. .Lpace)
  .Lpace%=:
    s_waitcnt lgkmcnt(0)
    s_barrier
    s_getpc_b64 s[4:5]
  .Lpc%=:
    s_sub_u32 s4, s4, .Lpc%=-.LKhead%=
    s_subb_u32 s5, s5, 0
    v_and_b32 v[.LT0], 63, %[tid]
    v_lshlrev_b32 v[.LT0], 7, v[.LT0]
    v_min_u32 v[.LT0], .Lpace%=-.LKhead%=, v[.LT0]
    global_load_dword v[.LT1], v[.LT0], s[4:5]
    s_lshl_b32 s21, %[bx], 21                               ; ... and warm the address translations of this workgroup's V and output
    s_add_u32 s12, s12, s21
    s_addc_u32 s13, s13, 0
    s_lshl_b32 s21, %[bx], 11
    v_mov_b32 v[.LT0], 0
    v_mov_b32 v[.LKO], s21
    global_load_dword v[.LOO], v[.LT0], s[12:13] nt
    global_load_dword v[.LKO+1], v[.LKO], s[14:15] nt
    s_waitcnt vmcnt(0)
  .Lend%=:
    )ASM"
      :
      : [ka] "s"(__builtin_amdgcn_kernarg_segment_ptr()), [bx] "s"(((blockIdx.x << 2) | (blockIdx.x >> 6)) & 255), [tid] "v"(threadIdx.x), [ldsp] "s"((LAS float*)lds), [ctl] "n"(SCORE_WORDS * 4)
      : "memory", "vcc", "scc",
#define V8(a, b, c, d, e, f, g, h) "v" #a, "v" #b, "v" #c, "v" #d, "v" #e, "v" #f, "v" #g, "v" #h
#define V10(t) "v" #t "0", "v" #t "1", "v" #t "2", "v" #t "3", "v" #t "4", "v" #t "5", "v" #t "6", "v" #t "7", "v" #t "8", "v" #t "9"
        V8(1, 2, 3, 4, 5, 6, 7, 8), "v9", V10(1), V10(2), V10(3), V10(4), V10(5), V10(6), V10(7), V10(8), V10(9), V10(10), V10(11), V10(12), V10(13), V10(14), V10(15),
        V10(16), V10(17), V10(18), V10(19), V10(20), V10(21), V10(22), "v230", "v231", "v232", "v233",
        "s4", "s5", "s6", "s7", "s8", "s9", "s10", "s11", "s12", "s13", "s14", "s15", "s16", "s17", "s18", "s19", "s20", "s21", "s22", "s23", "s24", "s25");
  if (__builtin_amdgcn_readfirstlane(threadIdx.x) < 64) return;

  const int lane = threadIdx.x & 63, bx = blockIdx.x;
  volatile LAS int* ctl = (volatile LAS int*)(lds + SCORE_WORDS);
  const auto rs = __builtin_amdgcn_make_buffer_rsrc((void*)ws, 0, 2 * NPUB * 128, 0x00020000);
  int next = MS0, m0 = 0, t0 = 0, t1 = 0, t2 = 0;
  int want = STEP, read_at = STEP + DLY + ((bx >> 3) & 7);
  int T = 0, P = -1;
  float tau = 0.f;
  for (;;) {
    __builtin_amdgcn_s_sleep(8);
    const unsigned long long pt = *(volatile LAS unsigned long long*)ctl;
    const int p = __builtin_amdgcn_readfirstlane((int)pt), t = __builtin_amdgcn_readfirstlane((int)(pt >> 32));
    if (p >= PEND) break;
    if (p >= next) {
      t2 = t1; t1 = t0; t0 = t - (p - next) * TAU0; m0 = next; next = (next / STEP + 1) * STEP;
      if (bx < NPUB && m0 >= STEP && lane == 0)
        __builtin_amdgcn_raw_buffer_store_b128(u32x4{(unsigned)m0, (unsigned)t1, (unsigned)t0, 0u}, rs, (((m0 / STEP) & 1) * NPUB + bx) * 128, 0, 16  );
    }
    if (p >= read_at) {
      const int tc = m0 == want ? t0 : t1, tp = m0 == want ? t1 : t2;
      const u32x4 m = __builtin_amdgcn_raw_buffer_load_b128(rs, lane < NPUB ? (((want / STEP) & 1) * NPUB + lane) * 128 : 0x10000, 0, 16  );
      const int d0 = (int)m[1] - tp, d1 = (int)m[2] - tc;
      const bool ok = (int)m[0] == want && d0 > -NEAR && d0 < NEAR && d1 > -NEAR && d1 < NEAR;
      const int s0 = sum16(ok ? d0 : 0), s1 = sum16(ok ? d1 : 0), n = sum16(ok ? 1 : 0);
      if (n > 0 && (m0 == want || m0 == want + STEP)) {
        const float inv = 1.f / (float)n, a0 = (float)s0 * inv, a1 = (float)s1 * inv;
        const float tn = ((float)(tc - tp) + a1 - a0) * (want == STEP ? 1.f / (STEP - MS0) : 1.f / STEP);
        tau = P < 0 ? tn : tau + FILT * fminf(fmaxf(tn - tau, -CLMP), CLMP);
        T = tc + (int)a1; P = want;
      }
      want += STEP; read_at += STEP;
    }
    if (P >= 0) {
      const int late = t - T - (int)((float)(p - P) * tau);
      ctl[2] = late > TH ? 0 : late < -TH ? 2 : 1;
    }
  }
}
}

extern "C" void kernel_launch(void* const* d_in, const int* in_sizes, int n_in, void* d_out, int out_size, void* d_ws, size_t ws_size, hipStream_t stream) {
  attn_k<<<NBH / HPW, 128, 0, stream>>>((const char*)d_in[1], (unsigned)NBH * SKV * D * 2, 0x00020000u, (const char*)d_in[0], (unsigned)NBH * SQ * D * 2, 0x00020000u,
                                        (const char*)d_in[2], (char*)d_out, (char*)d_ws);
}
```

```cpp
#include <hip/hip_runtime.h>
#include <cstdint>

namespace {
constexpr int D = 128, SKV = 4096, SQ = 4, NBH = 512, HPW = 2;
constexpr int SCORE_WORDS = HPW * SKV * SQ;
constexpr int NPUB = 8, MS0 = 4, STEP = 12, DLY = 4, PEND = 126, TAU0 = 110, TH = 25, NEAR = 2000;
constexpr float FILT = 0.7f, CLMP = 3.f;
using u32x4 = __attribute__((ext_vector_type(4))) unsigned;
#define LAS __attribute__((address_space(3)))
template <int CTRL> __device__ __forceinline__ int dpp(int x) { return __builtin_amdgcn_mov_dpp(x, CTRL, 0xf, 0xf, true); }
__device__ __forceinline__ int sum16(int x) { x += dpp<0xB1>(x); x += dpp<0x4E>(x); x += dpp<0x141>(x); x += dpp<0x128>(x); return __builtin_amdgcn_readfirstlane(x); }

__global__ void __launch_bounds__(128, 1) attn_k(const char* k, unsigned kn, unsigned kc, const char* q, unsigned qn, unsigned qc, const char* v, char* out, char* ws) {
  __shared__ __attribute__((aligned(16))) float lds[SCORE_WORDS + 4];
  asm volatile(R"ASM(
    ; ---- vector registers
    .set .LT0, 1      ; temporaries
    .set .LT1, 2
    .set .LOO, 3      ; lane * 16
    .set .LKO, 4      ; 4 per-lane offsets of a tile's loads
    .set .LRG, 8      ; ring: 32 loads x 4
    .set .LQR, 136    ; Q fragments [head][line] x 4
    .set .LBQ, 152    ; B operands [line][tile of the pair] x 4
    .set .LDA, 168    ; scores of keys 0..7  [pair] x 4
    .set .LDB, 176    ; scores of keys 8..15 [pair] x 4
    .set .LRR, 184    ; scores of 4 tiles
    .set .LSC, 188    ; scores read back [2] x 4
    .set .LTT, 196    ; packed p
    .set .LPA, 200    ; A operands of P V, 4 x 2
    .set .LA0, 208    ; O[q][8c + 0..3]
    .set .LA1, 212    ; O[q][8c + 4..7]
    .set .LO0, 216    ; packed output of head 0
    .set .LPG, 220    ; {round, time}
    .set .LMX, 222
    .set .LM0, 223    ; max * c of the head in work
    .set .LM1, 224    ; max * c of head 1
    .set .LLL, 226    ; l
    .set .LRL, 227    ; 1 / l
    .set .LLW, 228    ; LDS write address
    .set .LLR, 229    ; LDS read address
    .set .LMKX, 230
    .set .LMKY, 231
    .set .LMD, 232    ; pacing mode
    .set .LCA, 233    ; LDS address of {round, time, mode}
    ; ---- scalar registers: s[4:7] K (later V) descriptor, s[8:11] Q descriptor, s[12:13] v, s[14:15] out, s16 tile offset, s17 rounds left of this head,
    ;      s18 phase / rounds left, s19 mode, s20 c = log2(e)/sqrt(D), s21 temporary, s[22:23] clock, s[24:25] lanes of tile 0 of a pair,
    ;      s26 head pair

    ; Order of the keys inside a head (any order works, K and V use the same one; this one spreads a CU's loads in flight over the most HBM channels):
    ; address bits 8, 12, 13, 16 = key inside a tile, 14, 11, 9 = tile inside a round, 10, 15, 17, 18, 19 = round.
    .macro LOAD m                 ; ring slot m <- 1 KiB of the tile at s16; then step s16 to the next tile
      buffer_load_dwordx4 v[.LRG+4*(\m):.LRG+4*(\m)+3], v[.LKO+((\m)&3)], s[4:7], s16 offen nt
      .if ((\m)&7) == 3
        s_addk_i32 s16, 0x4000
      .elseif ((\m)&15) == 7
        s_addk_i32 s16, -0x3800
      .elseif (\m) == 15
        s_addk_i32 s16, -0x4600
      .elseif (\m) == 31
        s_or_b32 s16, s16, 0x17800
        s_addk_i32 s16, 0x400
        s_andn2_b32 s16, s16, 0x17a00
      .endif
    .endm
    .macro REFILL m               ; after a tile's MFMAs: its 4 loads in one burst once at most 28 / 21 / 17 loads (pacing mode 0 / 1 / 2) are in flight
      s_cbranch_vccnz 1f
      s_waitcnt vmcnt(21)
      s_cmp_eq_u32 s19, 1
      s_cbranch_scc1 1f
      s_waitcnt vmcnt(17)
    1:
      LOAD \m
      LOAD \m+1
      LOAD \m+2
      LOAD \m+3
    .endm
    .macro ROUND                  ; start of a round: report {round, time}, take the mode
      s_waitcnt lgkmcnt(0)
      v_readfirstlane_b32 s19, v[.LMD]
      v_add_u32 v[.LPG], 1, v[.LPG]
      v_mov_b32 v[.LPG+1], s22
      ds_write_b64 v[.LCA], v[.LPG:.LPG+1]
    .endm
    .macro MID
      s_memrealtime s[22:23]
      ds_read_b32 v[.LMD], v[.LCA] offset:8
    .endm
    .macro XROWS op, r            ; r = op over the four 16-lane rows
      v_mov_b32 v[.LT1], v[\r]
      s_nop 1
      v_permlane16_swap_b32 v[\r], v[.LT1]
      s_nop 1
      \op v[\r], v[\r], v[.LT1]
      v_mov_b32 v[.LT1], v[\r]
      s_nop 1
      v_permlane32_swap_b32 v[\r], v[.LT1]
      s_nop 1
      \op v[\r], v[\r], v[.LT1]
    .endm
    .macro KTILE m, pair, tp      ; 16 keys: ring slots m..m+3
      s_waitcnt vmcnt(28)
      .if \tp == 0
        v_mfma_f32_16x16x32_bf16 v[.LDA+4*\pair:.LDA+4*\pair+3], v[.LRG+4*(\m):.LRG+4*(\m)+3], v[.LBQ:.LBQ+3], 0
      .else
        v_mfma_f32_16x16x32_bf16 v[.LDA+4*\pair:.LDA+4*\pair+3], v[.LRG+4*(\m):.LRG+4*(\m)+3], v[.LBQ+4:.LBQ+7], v[.LDA+4*\pair:.LDA+4*\pair+3]
      .endif
      v_mfma_f32_16x16x32_bf16 v[.LDA+4*\pair:.LDA+4*\pair+3], v[.LRG+4*(\m)+4:.LRG+4*(\m)+7], v[.LBQ+8+4*\tp:.LBQ+11+4*\tp], v[.LDA+4*\pair:.LDA+4*\pair+3]
      .if \tp == 0
        v_mfma_f32_16x16x32_bf16 v[.LDB+4*\pair:.LDB+4*\pair+3], v[.LRG+4*(\m)+8:.LRG+4*(\m)+11], v[.LBQ:.LBQ+3], 0
      .else
        v_mfma_f32_16x16x32_bf16 v[.LDB+4*\pair:.LDB+4*\pair+3], v[.LRG+4*(\m)+8:.LRG+4*(\m)+11], v[.LBQ+4:.LBQ+7], v[.LDB+4*\pair:.LDB+4*\pair+3]
      .endif
      v_mfma_f32_16x16x32_bf16 v[.LDB+4*\pair:.LDB+4*\pair+3], v[.LRG+4*(\m)+12:.LRG+4*(\m)+15], v[.LBQ+8+4*\tp:.LBQ+11+4*\tp], v[.LDB+4*\pair:.LDB+4*\pair+3]
      REFILL \m
    .endm
    .macro KPAIR m, pair          ; two tiles -> columns 8*pair..8*pair+7 of RR
      KTILE \m, \pair, 0
      KTILE \m+4, \pair, 1
      s_nop 3                     ; + REFILL (>= 6 slots): >= 8 wait states after the last MFMA
      .irp x, 0, 1, 2, 3
        v_permlane32_swap_b32 v[.LDA+4*\pair+\x], v[.LDB+4*\pair+\x]
      .endr
      s_nop 1
      .irp x, 0, 1, 2, 3
        .if \pair == 0
          v_add_f32_dpp v[.LRR+\x], v[.LDB+\x], v[.LDA+\x] row_ror:8 row_mask:0xf bank_mask:0x3
        .else
          v_add_f32_dpp v[.LRR+\x], v[.LDA+4+\x], v[.LDB+4+\x] row_ror:8 row_mask:0xf bank_mask:0xc
        .endif
      .endr
    .endm
    .macro KGROUP g               ; 64 keys: lane (row r, column 8*pair + 4*tile + q), register x = score of key 4r+x
      KPAIR 16*\g, 0
      KPAIR 16*\g+8, 1
      ds_write_b128 v[.LLW], v[.LRR:.LRR+3] offset:1024*\g
      v_max3_f32 v[.LMX], v[.LMX], v[.LRR], v[.LRR+1]
      v_max3_f32 v[.LMX], v[.LMX], v[.LRR+2], v[.LRR+3]
    .endm
    .macro PGROUP g               ; scores of 64 keys, lane (row r, column 4q + tile) -> packed p, l
      .irp x, 0, 1, 2, 3
        v_fma_f32 v[.LSC+4*\g+\x], v[.LSC+4*\g+\x], s20, -v[.LM0]
      .endr
      .irp x, 0, 1, 2, 3
        v_exp_f32 v[.LSC+4*\g+\x], v[.LSC+4*\g+\x]
      .endr
      .irp x, 0, 1, 2, 3
        v_cvt_pk_bf16_f32 v[.LTT+\x], v[.LSC+4*\g+\x], v[.LSC+4*\g+\x]
      .endr
      v_add_f32 v[.LSC+4*\g], v[.LSC+4*\g], v[.LSC+4*\g+1]
      v_add_f32 v[.LSC+4*\g+2], v[.LSC+4*\g+2], v[.LSC+4*\g+3]
      v_add_f32 v[.LLL], v[.LLL], v[.LSC+4*\g]
      v_add_f32 v[.LLL], v[.LLL], v[.LSC+4*\g+2]
    .endm
    .macro VTILE g, t, last       ; 16 keys: ring slots 16g+4t .. +3; the last round only drains the ring
      .irp x, 0, 1, 2, 3
        v_and_b32_dpp v[.LPA+2*\x], v[.LTT+\x], v[.LMKX] quad_perm:[\t,\t,\t,\t] row_mask:0xf bank_mask:0xf
        v_and_b32_dpp v[.LPA+2*\x+1], v[.LTT+\x], v[.LMKY] quad_perm:[\t,\t,\t,\t] row_mask:0xf bank_mask:0xf
      .endr
      .if \last == 0
        s_waitcnt vmcnt(28)
      .endif
      .irp x, 0, 1, 2, 3
        .if \last
          s_waitcnt vmcnt(31-16*\g-4*\t-\x)
        .endif
        v_mfma_f32_16x16x16_bf16 v[.LA0:.LA0+3], v[.LPA+2*\x:.LPA+2*\x+1], v[.LRG+64*\g+16*\t+4*\x:.LRG+64*\g+16*\t+4*\x+1], v[.LA0:.LA0+3]
        v_mfma_f32_16x16x16_bf16 v[.LA1:.LA1+3], v[.LPA+2*\x:.LPA+2*\x+1], v[.LRG+64*\g+16*\t+4*\x+2:.LRG+64*\g+16*\t+4*\x+3], v[.LA1:.LA1+3]
      .endr
      .if \last == 0
        REFILL 16*\g+4*\t
      .endif
    .endm
    .macro VGROUP g, last
      VTILE \g, 0, \last
      VTILE \g, 1, \last
      VTILE \g, 2, \last
      VTILE \g, 3, \last
    .endm
    .macro RECIP                  ; RL = 1 / l of query lane>>4
      v_add_f32_dpp v[.LRL], v[.LLL], v[.LLL] quad_perm:[1,0,3,2] row_mask:0xf bank_mask:0xf
      s_nop 1
      v_add_f32_dpp v[.LRL], v[.LRL], v[.LRL] quad_perm:[2,3,0,1] row_mask:0xf bank_mask:0xf
      XROWS v_add_f32, .LRL
      v_rcp_f32 v[.LT1], v[.LRL]
      s_nop 2
      .irp x, 0, 1, 2, 3
        v_mov_b32_dpp v[.LRL], v[.LT1] row_newbcast:4*\x row_mask:1<<\x bank_mask:0xf
        s_nop 1
      .endr
    .endm
    .macro NORM                   ; TT = bf16(O / l)
      .irp x, 0, 1, 2, 3
        v_mul_f32 v[.LA0+\x], v[.LRL], v[.LA0+\x]
        v_mul_f32 v[.LA1+\x], v[.LRL], v[.LA1+\x]
      .endr
      .irp x, 0, 1, 2, 3
        v_cvt_pk_bf16_f32 v[.LTT+\x], v[.LA0+2*\x], v[.LA0+2*\x+1]
      .endr
    .endm

    ; ================= prologue: first loads as early as possible
    s_load_dwordx8 s[4:11], %[ka], 0x0
    s_load_dwordx4 s[12:15], %[ka], 0x20
    s_getpc_b64 s[16:17]                                   ; pull the code up to .Lpace into L2 (one dword per 64 B, values unused)
  .Lpc%=:
    v_lshlrev_b32 v[.LT0], 6, %[tid]
    v_min_u32 v[.LT0], .Lpace%=-.Lpc%=, v[.LT0]
    global_load_dword v[.LO0], v[.LT0], s[16:17]
    s_lshr_b32 s26, %[bx], 3                               ; head pair: bits 0, 1, 2 of the workgroup id (its XCD) -> bits 2, 6, 7 (speed only)
    s_and_b32 s21, s26, 28
    s_add_u32 s26, s26, s21
    s_and_b32 s21, %[bx], 1
    s_lshl2_add_u32 s26, s21, s26
    s_and_b32 s21, %[bx], 6
    s_lshl_b32 s21, s21, 5
    s_add_u32 s26, s26, s21
    v_readfirstlane_b32 s21, %[tid]
    v_and_b32 v[.LT0], 56, %[tid]
    v_and_b32 v[.LT1], 1, %[tid]
    v_lshlrev_b32 v[.LT0], 1, v[.LT0]                      ; 16-B chunk (lane >> 3) of the line
    v_lshl_or_b32 v[.LKO], v[.LT1], 8, v[.LT0]             ; key lane & 7; loads 0, 1 = both lines of keys 0..7, loads 2, 3 = keys 8..15
    v_and_b32 v[.LT1], 6, %[tid]
    s_lshl_b32 s16, s26, 21
    v_lshl_or_b32 v[.LKO], v[.LT1], 11, v[.LKO]
    v_add_u32 v[.LKO+1], 0x80, v[.LKO]
    v_add_u32 v[.LKO+2], 0x10000, v[.LKO]
    v_add_u32 v[.LKO+3], 0x10080, v[.LKO]
    s_cmp_ge_u32 s21, 64
    s_cbranch_scc1 .Lpace%=
    s_waitcnt lgkmcnt(0)
    .irp m, 0, 1, 2, 3
      LOAD \m
    .endr
    v_and_b32 v[.LT1], 3, %[tid]
    s_lshl_b32 s21, s26, 11
    v_lshl_or_b32 v[.LT0], v[.LT1], 8, v[.LT0]             ; query lane & 3
    buffer_load_dwordx4 v[.LQR:.LQR+3], v[.LT0], s[8:11], s21 offen
    buffer_load_dwordx4 v[.LQR+4:.LQR+7], v[.LT0], s[8:11], s21 offen offset:128
    buffer_load_dwordx4 v[.LQR+8:.LQR+11], v[.LT0], s[8:11], s21 offen offset:1024
    buffer_load_dwordx4 v[.LQR+12:.LQR+15], v[.LT0], s[8:11], s21 offen offset:1152
    .irp m, 4, 5, 6, 7, 8, 9, 10, 11, 12, 13, 14, 15, 16, 17, 18, 19, 20, 21, 22, 23, 24, 25, 26, 27, 28, 29, 30, 31
      LOAD \m
    .endr
    s_add_u32 s14, s14, s21
    s_addc_u32 s15, s15, 0
    s_mov_b32 s20, 0x3e0293ee
    v_lshlrev_b32 v[.LOO], 4, %[tid]
    v_and_b32 v[.LT0], 48, %[tid]
    v_add_u32 v[.LLR], %[ldsp], v[.LOO]
    v_lshlrev_b32 v[.LLW], 6, v[.LT1]                      ; LDS slot 16r + 4q + 2*pair + tile
    v_lshl_or_b32 v[.LLW], v[.LT0], 4, v[.LLW]
    v_and_b32 v[.LT0], 12, %[tid]
    v_lshl_or_b32 v[.LLW], v[.LT0], 2, v[.LLW]
    v_add_u32 v[.LLW], %[ldsp], v[.LLW]
    v_and_b32 v[.LT0], 4, %[tid]
    v_cmp_eq_u32 s[24:25], 0, v[.LT0]
    v_and_b32 v[.LT0], 1, %[tid]
    v_lshlrev_b32 v[.LT0], 4, v[.LT0]
    v_mov_b32 v[.LMX], 0xffff
    v_lshlrev_b32 v[.LT0], v[.LT0], v[.LMX]                ; element j0 = lane & 3 of the 4-element A fragment
    v_cmp_gt_u32 vcc, 2, v[.LT1]
    v_mov_b32 v[.LPG], 0
    v_mov_b32 v[.LMD], 1
    v_cndmask_b32 v[.LMKX], 0, v[.LT0], vcc
    v_cndmask_b32 v[.LMKY], v[.LT0], 0, vcc
    s_add_u32 s21, %[ldsp], %[ctl]
    v_mov_b32 v[.LCA], s21
    ds_write2_b32 v[.LCA], v[.LPG], v[.LMD] offset1:2
    s_memrealtime s[22:23]
    s_waitcnt lgkmcnt(0)
    s_barrier
    s_mov_b32 s17, 32
    s_mov_b32 s18, 0
    s_waitcnt vmcnt(28)

    ; ================= K phase: 32 rounds per head
  .LKhead%=:
    v_mov_b32 v[.LMX], 0xff7fffff
    .irp x, 0, 1, 2, 3
      v_cndmask_b32 v[.LBQ+\x], 0, v[.LQR+\x], s[24:25]
      v_cndmask_b32 v[.LBQ+4+\x], v[.LQR+\x], 0, s[24:25]
    .endr
    .irp x, 0, 1, 2, 3
      v_cndmask_b32 v[.LBQ+8+\x], 0, v[.LQR+4+\x], s[24:25]
      v_cndmask_b32 v[.LBQ+12+\x], v[.LQR+4+\x], 0, s[24:25]
    .endr
  .LK%=:
    ROUND
    s_cmp_eq_u32 s19, 0
    s_cselect_b64 vcc, -1, 0
    KGROUP 0
    MID
    KGROUP 1
    v_add_u32 v[.LLW], 0x800, v[.LLW]
    s_sub_u32 s17, s17, 1
    s_cmp_lg_u32 s17, 0
    s_cbranch_scc1 .LK%=
    s_cmp_eq_u32 s18, 1
    s_cbranch_scc0 .LKmax%=
    ; the last K round refills the ring with the first V tiles: load x of a tile = keys 4r + x
    s_lshl_b32 s21, s26, 21
    s_add_u32 s4, s12, s21
    s_addc_u32 s5, s13, 0
    s_mov_b32 s6, 0x200000
    s_mov_b32 s16, 0
    v_and_b32 v[.LKO], 0xf0, v[.LOO]
    v_and_b32 v[.LT0], 16, %[tid]
    v_lshl_or_b32 v[.LKO], v[.LT0], 9, v[.LKO]
    v_and_b32 v[.LT0], 32, %[tid]
    v_lshl_or_b32 v[.LKO], v[.LT0], 11, v[.LKO]
    v_add_u32 v[.LKO+1], 0x100, v[.LKO]
    v_add_u32 v[.LKO+2], 0x1000, v[.LKO]
    v_add_u32 v[.LKO+3], 0x1100, v[.LKO]
    s_mov_b32 s18, 2
    s_mov_b32 s17, 1
    s_branch .LK%=
  .LKmax%=:                                                 ; row maxima of the finished head (query = lane & 3) -> M1 (query = (lane >> 2) & 3)
    v_mov_b32 v[.LM0], v[.LM1]
    v_max_f32_dpp v[.LMX], v[.LMX], v[.LMX] row_ror:4 row_mask:0xf bank_mask:0xf
    s_nop 1
    v_max_f32_dpp v[.LMX], v[.LMX], v[.LMX] row_ror:8 row_mask:0xf bank_mask:0xf
    XROWS v_max_f32, .LMX
    v_mul_f32 v[.LMX], s20, v[.LMX]
    s_nop 1
    .irp x, 0, 1, 2, 3
      v_mov_b32_dpp v[.LM1], v[.LMX] row_newbcast:\x row_mask:0xf bank_mask:1<<\x
      s_nop 1
    .endr
    s_cmp_eq_u32 s18, 0
    s_cbranch_scc0 .LKdone%=
    .irp x, 0, 1, 2, 3, 4, 5, 6, 7
      v_mov_b32 v[.LQR+\x], v[.LQR+8+\x]
    .endr
    s_mov_b32 s18, 1
    s_mov_b32 s17, 31
    s_branch .LKhead%=

    ; ================= V phase: 32 rounds per head, the very last one peeled
  .LKdone%=:
    ds_read_b128 v[.LSC:.LSC+3], v[.LLR]
    s_mov_b32 s18, 63
    s_mov_b32 s17, 32
  .LVhead%=:
    v_mov_b32 v[.LLL], 0
    .irp x, 0, 1, 2, 3
      v_mov_b32 v[.LA0+\x], 0
      v_mov_b32 v[.LA1+\x], 0
    .endr
  .LV%=:
    ROUND
    s_cmp_le_u32 s18, 1
    s_cselect_b32 s19, 0, s19                               ; the round that issues the last loads is not throttled
    s_sub_u32 s18, s18, 1
    s_cmp_eq_u32 s19, 0
    s_cselect_b64 vcc, -1, 0
    ds_read_b128 v[.LSC+4:.LSC+7], v[.LLR] offset:1024
    PGROUP 0
    VGROUP 0, 0
    s_waitcnt lgkmcnt(0)
    ds_read_b128 v[.LSC:.LSC+3], v[.LLR] offset:2048
    MID
    PGROUP 1
    VGROUP 1, 0
    v_add_u32 v[.LLR], 0x800, v[.LLR]
    s_sub_u32 s17, s17, 1
    s_cmp_lg_u32 s17, 0
    s_cbranch_scc1 .LV%=
    s_cmp_eq_u32 s18, 0
    s_cbranch_scc1 .LVlast%=
    RECIP
    NORM
    .irp x, 0, 1, 2, 3
      v_mov_b32 v[.LO0+\x], v[.LTT+\x]
    .endr
    v_mov_b32 v[.LM0], v[.LM1]
    s_mov_b32 s17, 31
    s_branch .LVhead%=
  .LVlast%=:
    s_waitcnt lgkmcnt(0)
    ds_read_b128 v[.LSC+4:.LSC+7], v[.LLR] offset:1024
    PGROUP 0
    VGROUP 0, 1
    s_waitcnt lgkmcnt(0)
    PGROUP 1
    s_nop 1
    RECIP
    VGROUP 1, 1
    global_store_dwordx4 v[.LOO], v[.LO0:.LO0+3], s[14:15]
    s_nop 6
    NORM
    global_store_dwordx4 v[.LOO], v[.LTT:.LTT+3], s[14:15] offset:1024
    s_waitcnt vmcnt(0)
    s_branch .Lend%=

    ; ================= pacing wave
  .Lpace%=:
    s_waitcnt lgkmcnt(0)
    s_barrier
    s_lshl_b32 s21, s26, 21                                ; warm the address translations of this workgroup's V and output (values unused)
    v_mov_b32 v[.LKO], s21
    s_lshl_b32 s21, s26, 11
    v_mov_b32 v[.LKO+1], s21
    global_load_dword v[.LKO+2], v[.LKO], s[12:13] nt
    global_load_dword v[.LKO+3], v[.LKO+1], s[14:15] nt
    s_waitcnt vmcnt(0)
  .Lend%=:
    )ASM"
      :
      : [ka] "s"(__builtin_amdgcn_kernarg_segment_ptr()), [bx] "s"(blockIdx.x), [tid] "v"(threadIdx.x), [ldsp] "s"((LAS float*)lds), [ctl] "n"(SCORE_WORDS * 4)
      : "memory", "vcc", "scc",
#define V8(a, b, c, d, e, f, g, h) "v" #a, "v" #b, "v" #c, "v" #d, "v" #e, "v" #f, "v" #g, "v" #h
#define V10(t) "v" #t "0", "v" #t "1", "v" #t "2", "v" #t "3", "v" #t "4", "v" #t "5", "v" #t "6", "v" #t "7", "v" #t "8", "v" #t "9"
        V8(1, 2, 3, 4, 5, 6, 7, 8), "v9", V10(1), V10(2), V10(3), V10(4), V10(5), V10(6), V10(7), V10(8), V10(9), V10(10), V10(11), V10(12), V10(13), V10(14), V10(15),
        V10(16), V10(17), V10(18), V10(19), V10(20), V10(21), V10(22), "v230", "v231", "v232", "v233",
        "s4", "s5", "s6", "s7", "s8", "s9", "s10", "s11", "s12", "s13", "s14", "s15", "s16", "s17", "s18", "s19", "s20", "s21", "s22", "s23", "s24", "s25", "s26");
  if (__builtin_amdgcn_readfirstlane(threadIdx.x) < 64) return;

  const int lane = threadIdx.x & 63, bx = blockIdx.x;
  volatile LAS int* ctl = (volatile LAS int*)(lds + SCORE_WORDS);
  const auto rs = __builtin_amdgcn_make_buffer_rsrc((void*)ws, 0, 2 * NPUB * 128, 0x00020000);
  int next = MS0, m0 = 0, t0 = 0, t1 = 0, t2 = 0;
  int want = STEP, read_at = STEP + DLY + ((bx >> 3) & 7);
  int T = 0, P = -1;
  float tau = 0.f;
  for (;;) {
    __builtin_amdgcn_s_sleep(8);
    const unsigned long long pt = *(volatile LAS unsigned long long*)ctl;
    const int p = __builtin_amdgcn_readfirstlane((int)pt), t = __builtin_amdgcn_readfirstlane((int)(pt >> 32));
    if (p >= PEND) break;
    if (p >= next) {
      t2 = t1; t1 = t0; t0 = t - (p - next) * TAU0; m0 = next; next = (next / STEP + 1) * STEP;
      if (bx < NPUB && m0 >= STEP && lane == 0)
        __builtin_amdgcn_raw_buffer_store_b128(u32x4{(unsigned)m0, (unsigned)t1, (unsigned)t0, 0u}, rs, (((m0 / STEP) & 1) * NPUB + bx) * 128, 0, 16  );
    }
    if (p >= read_at) {
      const int tc = m0 == want ? t0 : t1, tp = m0 == want ? t1 : t2;
      const u32x4 m = __builtin_amdgcn_raw_buffer_load_b128(rs, lane < NPUB ? (((want / STEP) & 1) * NPUB + lane) * 128 : 0x10000, 0, 16  );
      const int d0 = (int)m[1] - tp, d1 = (int)m[2] - tc;
      const bool ok = (int)m[0] == want && d0 > -NEAR && d0 < NEAR && d1 > -NEAR && d1 < NEAR;
      const int s0 = sum16(ok ? d0 : 0), s1 = sum16(ok ? d1 : 0), n = sum16(ok ? 1 : 0);
      if (n > 0 && (m0 == want || m0 == want + STEP)) {
        const float inv = 1.f / (float)n, a0 = (float)s0 * inv, a1 = (float)s1 * inv;
        const float tn = ((float)(tc - tp) + a1 - a0) * (want == STEP ? 1.f / (STEP - MS0) : 1.f / STEP);
        tau = P < 0 ? tn : tau + FILT * fminf(fmaxf(tn - tau, -CLMP), CLMP);
        T = tc + (int)a1; P = want;
      }
      want += STEP; read_at += STEP;
    }
    if (P >= 0) {
      const int late = t - T - (int)((float)(p - P) * tau);
      ctl[2] = late > TH ? 0 : late < -TH ? 2 : 1;
    }
  }
}
}

extern "C" void kernel_launch(void* const* d_in, const int* in_sizes, int n_in, void* d_out, int out_size, void* d_ws, size_t ws_size, hipStream_t stream) {
  attn_k<<<NBH / HPW, 128, 0, stream>>>((const char*)d_in[1], (unsigned)NBH * SKV * D * 2, 0x00020000u, (const char*)d_in[0], (unsigned)NBH * SQ * D * 2, 0x00020000u,
                                        (const char*)d_in[2], (char*)d_out, (char*)d_ws);
}
```
